```python
import torch

def _softmax(x, axis=-1):
    return torch.softmax(x, dim=axis)
def _gelu(x):
    return torch.nn.functional.gelu(x, approximate="tanh")
def _silu(x):
    return torch.nn.functional.silu(x)
def _sigmoid(x):
    return torch.sigmoid(x)
def _layernorm(x, axis=-1, eps=1e-5):
    m = x.mean(dim=axis, keepdim=True)
    v = x.var(dim=axis, keepdim=True, unbiased=False)
    return (x - m) / torch.sqrt(v + eps)
def _scatter_add(tgt, idx, src, axis=0):
    return tgt.clone().index_add_(axis, idx, src)
def _segment_sum(src, seg, n):
    out = torch.zeros((n,) + src.shape[1:], dtype=src.dtype)
    return out.index_add_(0, seg, src)
def _pad(x, pw):
    flat = tuple(v for pair in reversed(pw) for v in pair)
    return torch.nn.functional.pad(x, flat)

kernel_name = 'standalone__mha_block_cached__near_ridge'

def setup_inputs():

    _w = (lambda a: (lambda t: t.to(torch.bfloat16) if t.is_floating_point() else t)(torch.as_tensor(a)))
    B, Sq, D = 8, 64, 1024
    H, Sk, Dh = 32, 64, 128
    return {
        "x": _w((lambda *s: torch.randn(*s, dtype=torch.bfloat16) * 0.8)(B, Sq, D)),
        "k_cache": _w((lambda *s: torch.randn(*s, dtype=torch.bfloat16) * 0.8)(B, H, Sk, Dh)),
        "v_cache": _w((lambda *s: torch.randn(*s, dtype=torch.bfloat16) * 0.05)(B, H, Sk, Dh)),
        "wq": _w((lambda *s: torch.randn(*s, dtype=torch.bfloat16) * 0.8)(D, H * Dh) * (D ** -0.5)),
        "wo": _w((lambda *s: torch.randn(*s, dtype=torch.bfloat16) * 0.05)(H * Dh, D) * ((H * Dh) ** -0.5)),
    }

def reference(x, k_cache, v_cache, wq, wo):
    B, Sq, D = x.shape
    H, Dh = 32, 128
    q = (lambda x, *d: x.permute(*d))((x @ wq).reshape(B, Sq, H, Dh), 0, 2, 1, 3)
    scores = torch.einsum("bhqd,bhkd->bhqk", q, k_cache) * (Dh ** -0.5)
    p = _softmax(scores, axis=-1)
    o = torch.einsum("bhqk,bhkd->bhqd", p, v_cache)
    return (lambda x, *d: x.permute(*d))(o, 0, 2, 1, 3).reshape(B, Sq, H * Dh) @ wo
```

```cpp
#include <hip/hip_runtime.h>
#include <hip/hip_bf16.h>
typedef __hip_bfloat16 bf16;
__device__ __forceinline__ float b2f(bf16 x){return __bfloat162float(x);}
__global__ void k_q(const bf16* x,const bf16* wq,bf16* q){
  int n=blockIdx.x*64+(threadIdx.x&63); int m=blockIdx.y*4+(threadIdx.x>>6);
  float acc=0; for(int k=0;k<1024;++k) acc+=b2f(x[m*1024+k])*b2f(wq[(size_t)k*4096+n]);
  q[(size_t)m*4096+n]=__float2bfloat16(acc);
}
__global__ void k_attn(const bf16* q,const bf16* kc,const bf16* vc,bf16* o){
  int bh=blockIdx.x,b=bh/32,h=bh%32; int s=threadIdx.x;
  __shared__ float p[64][65];
  const bf16* qr=q+(size_t)(b*64+s)*4096+h*128;
  float mx=-1e30f;
  for(int j=0;j<64;++j){ float a=0; const bf16* kr=kc+((size_t)bh*64+j)*128; for(int d=0;d<128;++d)a+=b2f(qr[d])*b2f(kr[d]);
    a=b2f(__float2bfloat16(a)); a=b2f(__float2bfloat16(a*0.08838834764831845f)); p[s][j]=a; mx=fmaxf(mx,a);}
  float sum=0; for(int j=0;j<64;++j){float e=__expf(p[s][j]-mx); p[s][j]=e; sum+=e;}
  for(int j=0;j<64;++j) p[s][j]=b2f(__float2bfloat16(p[s][j]/sum));
  for(int d=0;d<128;++d){ float a=0; for(int j=0;j<64;++j)a+=p[s][j]*b2f(vc[((size_t)bh*64+j)*128+d]); o[(size_t)(b*64+s)*4096+h*128+d]=__float2bfloat16(a);}
}
__global__ void k_o(const bf16* o,const bf16* wo,bf16* out){
  int n=blockIdx.x*64+(threadIdx.x&63); int m=blockIdx.y*4+(threadIdx.x>>6);
  float acc=0; for(int k=0;k<4096;++k) acc+=b2f(o[(size_t)m*4096+k])*b2f(wo[(size_t)k*1024+n]);
  out[(size_t)m*1024+n]=__float2bfloat16(acc);
}
extern "C" void kernel_launch(void* const* d_in, const int* in_sizes, int n_in, void* d_out, int out_size, void* d_ws, size_t ws_size, hipStream_t stream){
  const bf16* x=(const bf16*)d_in[0]; const bf16* kc=(const bf16*)d_in[1]; const bf16* vc=(const bf16*)d_in[2];
  const bf16* wq=(const bf16*)d_in[3]; const bf16* wo=(const bf16*)d_in[4];
  bf16* q=(bf16*)d_ws; bf16* o=q+512*4096;
  k_q<<<dim3(64,128),256,0,stream>>>(x,wq,q);
  k_attn<<<256,64,0,stream>>>(q,kc,vc,o);
  k_o<<<dim3(16,128),256,0,stream>>>(o,wo,(bf16*)d_out);
}
```

```cpp
#include <hip/hip_runtime.h>
#include <hip/hip_bf16.h>
#include <utility>

typedef __attribute__((ext_vector_type(8))) short bf16x8;
typedef __attribute__((ext_vector_type(4))) short bf16x4;
typedef __attribute__((ext_vector_type(4))) float f32x4;
typedef __attribute__((ext_vector_type(16))) float f32x16;
typedef __attribute__((ext_vector_type(4))) unsigned u32x4;
typedef __attribute__((ext_vector_type(2))) unsigned u32x2;
#define LDS_AS __attribute__((address_space(3)))

#define WAITV(n) asm volatile("s_waitcnt vmcnt(%0)" ::"n"(n) : "memory")
#define WAITL0() asm volatile("s_waitcnt lgkmcnt(0)" ::: "memory")
#define BARRIER() do { asm volatile("" ::: "memory"); __builtin_amdgcn_s_barrier(); asm volatile("" ::: "memory"); } while (0)

__device__ __forceinline__ unsigned pack_bf16(float lo, float hi) {
  typedef __attribute__((ext_vector_type(2))) float f32x2;
  typedef __attribute__((ext_vector_type(2))) __bf16 bf16x2;
  f32x2 v = {lo, hi};
  bf16x2 r = __builtin_convertvector(v, bf16x2);
  return __builtin_bit_cast(unsigned, r);
}
__device__ __forceinline__ unsigned lds_addr(const char* p) { return (unsigned)(size_t)(LDS_AS const char*)p; }
__device__ __forceinline__ void dma16(const char* gsrc, char* lds_base  ) {
  unsigned keep;
  unsigned dst = lds_addr(lds_base);
  asm volatile("s_mov_b32 %0, m0\n\ts_mov_b32 m0, %2\n\ts_nop 0\n\tglobal_load_lds_dwordx4 %1, off\n\ts_mov_b32 m0, %0"
               : "=&s"(keep) : "v"(gsrc), "s"(dst) : "memory");
}
__device__ __forceinline__ bf16x8 lds_rd128(const char* p) { return *(const bf16x8*)p; }
__device__ __forceinline__ bf16x4 lds_tr(const char* p) {
  return __builtin_amdgcn_ds_read_tr16_b64_v4i16((LDS_AS bf16x4*)p);
}
__device__ __forceinline__ bf16x8 cat4(bf16x4 a, bf16x4 b) {
  return __builtin_shufflevector(a, b, 0, 1, 2, 3, 4, 5, 6, 7);
}

template <int... I, class F>
__device__ __forceinline__ void static_for_impl(std::integer_sequence<int, I...>, F&& f) {
  (f(std::integral_constant<int, I>{}), ...);
}
template <int N, class F>
__device__ __forceinline__ void static_for(F&& f) { static_for_impl(std::make_integer_sequence<int, N>{}, f); }

constexpr int K1_NS = 6;
constexpr int K1_SLOT = 24576;
constexpr int K1_NT = 16;

__global__ __launch_bounds__(256) void qattn_kernel(const __hip_bfloat16* __restrict__ x,
                                                    const __hip_bfloat16* __restrict__ kc,
                                                    const __hip_bfloat16* __restrict__ vc,
                                                    const __hip_bfloat16* __restrict__ wq,
                                                    __hip_bfloat16* __restrict__ o) {
  __shared__ __attribute__((aligned(1024))) char smem[K1_NS * K1_SLOT];
  const int tid = threadIdx.x, lane = tid & 63;
  const int w = __builtin_amdgcn_readfirstlane(tid >> 6);
  const int bid = blockIdx.x;
  const int h = (bid & 7) * 4 + ((bid >> 3) & 3);
  const int b = bid >> 5;

  const char* xsrc[2];
  const char* wsrc[4];
#pragma unroll
  for (int i = 0; i < 2; ++i) {
    int row = 8 * (w * 2 + i) + (lane >> 3);
    int c = (lane & 7) ^ ((row >> 1) & 7);
    xsrc[i] = (const char*)x + ((size_t)(b * 64 + row) * 1024) * 2 + c * 16;
  }
#pragma unroll
  for (int i = 0; i < 4; ++i) {
    int row = 4 * (w * 4 + i) + (lane >> 4);
    int c = (lane & 15) ^ ((row & 3) << 2);
    wsrc[i] = (const char*)wq + ((size_t)row * 4096 + h * 128) * 2 + c * 16;
  }
  auto issue_stage = [&](int t) {
    char* slot = smem + (t % K1_NS) * K1_SLOT;
#pragma unroll
    for (int i = 0; i < 2; ++i) dma16(xsrc[i] + (size_t)t * 128, slot + (w * 2 + i) * 1024);
#pragma unroll
    for (int i = 0; i < 4; ++i) dma16(wsrc[i] + (size_t)t * 64 * 8192, slot + 8192 + (w * 4 + i) * 1024);
  };
  char* const vimg = smem + 4 * K1_SLOT;
  char* const kimg = smem + 5 * K1_SLOT;
  char* const qimg = smem + 0 * K1_SLOT;
  auto issue_v = [&]() {
    const char* vb = (const char*)vc + (size_t)(b * 32 + h) * 64 * 128 * 2;
#pragma unroll
    for (int i = 0; i < 4; ++i) {
      int p = w * 4 + i;
      int row = 4 * p + (lane >> 4);
      int c = (lane & 15) ^ ((row & 7) << 1);
      dma16(vb + row * 256 + c * 16, vimg + p * 1024);
    }
  };
  auto issue_k = [&]() {
    const char* kb = (const char*)kc + (size_t)(b * 32 + h) * 64 * 128 * 2;
#pragma unroll
    for (int i = 0; i < 4; ++i) {
      int p = w * 4 + i;
      int row = 4 * p + (lane >> 4);
      int c = (lane & 15) ^ (row & 15);
      dma16(kb + row * 256 + c * 16, kimg + p * 1024);
    }
  };

  const int wd = w >> 1, wqh = w & 1;
  int xoff[4], woff[2][4][2];
#pragma unroll
  for (int ks = 0; ks < 4; ++ks) {
    int xr = 32 * wqh + (lane & 31);
    int ch = 2 * ks + (lane >> 5);
    xoff[ks] = xr * 128 + ((ch ^ ((xr >> 1) & 7)) << 4);
#pragma unroll
    for (int i = 0; i < 2; ++i)
#pragma unroll
      for (int t = 0; t < 2; ++t) {
        int db = 2 * wd + i;
        int row = 16 * ks + 8 * (lane >> 5) + 4 * t + ((lane & 15) >> 2);
        int ch2 = 4 * db + 2 * ((lane >> 4) & 1) + ((lane & 3) >> 1);
        woff[i][ks][t] = 8192 + row * 256 + ((ch2 ^ ((row & 3) << 2)) << 4) + 8 * (lane & 1);
      }
  }

  f32x16 acc[2];
#pragma unroll
  for (int i = 0; i < 2; ++i)
#pragma unroll
    for (int r = 0; r < 16; ++r) acc[i][r] = 0.f;

#pragma unroll
  for (int t = 0; t < K1_NS - 1; ++t) issue_stage(t);

  static_for<K1_NT>([&](auto tc) {
    constexpr int t = decltype(tc)::value;
    constexpr int allowed = (t <= 11) ? 24 : (t == 12) ? 22 : (t == 13) ? 20 : (t == 14) ? 14 : 8;
    WAITV(allowed);
    BARRIER();
    if constexpr (t + K1_NS - 1 < K1_NT) issue_stage(t + K1_NS - 1);
    else if constexpr (t == 11) issue_v();
    else if constexpr (t == 12) issue_k();
    const char* slot = smem + (t % K1_NS) * K1_SLOT;
#pragma unroll
    for (int ks = 0; ks < 4; ++ks) {
      bf16x8 xf = lds_rd128(slot + xoff[ks]);
#pragma unroll
      for (int i = 0; i < 2; ++i) {
        bf16x8 wf = cat4(lds_tr(slot + woff[i][ks][0]), lds_tr(slot + woff[i][ks][1]));
        acc[i] = __builtin_amdgcn_mfma_f32_32x32x16_bf16(wf, xf, acc[i], 0, 0, 0);
      }
    }
  });

  {
    int qrow = 32 * wqh + (lane & 31);
#pragma unroll
    for (int i = 0; i < 2; ++i)
#pragma unroll
      for (int g = 0; g < 4; ++g) {
        int ch = 4 * (2 * wd + i) + g;
        u32x2 v;
        v[0] = pack_bf16(acc[i][4 * g + 0], acc[i][4 * g + 1]);
        v[1] = pack_bf16(acc[i][4 * g + 2], acc[i][4 * g + 3]);
        *(u32x2*)(qimg + qrow * 256 + ((ch ^ (qrow & 15)) << 4) + 8 * (lane >> 5)) = v;
      }
  }
  asm volatile("s_waitcnt vmcnt(0) lgkmcnt(0)" ::: "memory");
  BARRIER();

  const int l15 = lane & 15, g = lane >> 4;
  bf16x8 qf[4];
  {
    int qr = 16 * w + l15;
#pragma unroll
    for (int s = 0; s < 4; ++s) qf[s] = lds_rd128(qimg + qr * 256 + (((4 * s + g) ^ (qr & 15)) << 4));
  }
  f32x4 st[4];
#pragma unroll
  for (int kb = 0; kb < 4; ++kb) {
    st[kb] = (f32x4){0.f, 0.f, 0.f, 0.f};
    int kr = 16 * kb + l15;
#pragma unroll
    for (int s = 0; s < 4; ++s) {
      bf16x8 kf = lds_rd128(kimg + kr * 256 + (((4 * s + g) ^ (kr & 15)) << 4));
      st[kb] = __builtin_amdgcn_mfma_f32_16x16x32_bf16(kf, qf[s], st[kb], 0, 0, 0);
    }
  }
  const float scale = 0.08838834764831845f;
  float mx = -3.0e38f;
#pragma unroll
  for (int kb = 0; kb < 4; ++kb)
#pragma unroll
    for (int r = 0; r < 4; ++r) { st[kb][r] *= scale; mx = fmaxf(mx, st[kb][r]); }
  mx = fmaxf(mx, __shfl_xor(mx, 16));
  mx = fmaxf(mx, __shfl_xor(mx, 32));
  float sum = 0.f;
#pragma unroll
  for (int kb = 0; kb < 4; ++kb)
#pragma unroll
    for (int r = 0; r < 4; ++r) { float e = __expf(st[kb][r] - mx); st[kb][r] = e; sum += e; }
  sum += __shfl_xor(sum, 16);
  sum += __shfl_xor(sum, 32);
  const float rinv = 1.0f / sum;
  bf16x8 pf[2];
#pragma unroll
  for (int s2 = 0; s2 < 2; ++s2) {
    u32x4 u;
    u[0] = pack_bf16(st[2 * s2][0] * rinv, st[2 * s2][1] * rinv);
    u[1] = pack_bf16(st[2 * s2][2] * rinv, st[2 * s2][3] * rinv);
    u[2] = pack_bf16(st[2 * s2 + 1][0] * rinv, st[2 * s2 + 1][1] * rinv);
    u[3] = pack_bf16(st[2 * s2 + 1][2] * rinv, st[2 * s2 + 1][3] * rinv);
    pf[s2] = __builtin_bit_cast(bf16x8, u);
  }
  __hip_bfloat16* orow = o + (size_t)(b * 64 + 16 * w + l15) * 4096 + h * 128;
#pragma unroll
  for (int db = 0; db < 8; ++db) {
    f32x4 oa = {0.f, 0.f, 0.f, 0.f};
#pragma unroll
    for (int s2 = 0; s2 < 2; ++s2) {
      bf16x4 vv[2];
#pragma unroll
      for (int t = 0; t < 2; ++t) {
        int row = 32 * s2 + 16 * t + 4 * g + (l15 >> 2);
        int ch = 2 * db + ((lane & 3) >> 1);
        vv[t] = lds_tr(vimg + row * 256 + ((ch ^ ((row & 7) << 1)) << 4) + 8 * (lane & 1));
      }
      oa = __builtin_amdgcn_mfma_f32_16x16x32_bf16(cat4(vv[0], vv[1]), pf[s2], oa, 0, 0, 0);
    }
    u32x2 v;
    v[0] = pack_bf16(oa[0], oa[1]);
    v[1] = pack_bf16(oa[2], oa[3]);
    *(u32x2*)(orow + 16 * db + 4 * g) = v;
  }
}

constexpr int K2_NS = 6;
constexpr int K2_SLOT = 24576;
constexpr int K2_NT = 32;

__global__ __launch_bounds__(256) void oproj_kernel(const __hip_bfloat16* __restrict__ o,
                                                    const __hip_bfloat16* __restrict__ wo,
                                                    __hip_bfloat16* __restrict__ out) {
  __shared__ __attribute__((aligned(1024))) char smem[K2_NS * K2_SLOT];
  const int tid = threadIdx.x, lane = tid & 63;
  const int w = __builtin_amdgcn_readfirstlane(tid >> 6);
  const int bid = blockIdx.x;
  const int xj = bid & 7, idx = bid >> 3;
  const int mt = 8 * (xj >> 2) + (idx >> 2);
  const int nt = 4 * (xj & 3) + (idx & 3);
  const int m0 = 32 * mt, n0 = 64 * nt;

  const char* asrc[2];
  const char* bsrc[4];
#pragma unroll
  for (int i = 0; i < 2; ++i) {
    int row = 4 * (w * 2 + i) + (lane >> 4);
    int c = (lane & 15) ^ (row & 15);
    asrc[i] = (const char*)o + ((size_t)(m0 + row) * 4096) * 2 + c * 16;
  }
#pragma unroll
  for (int i = 0; i < 4; ++i) {
    int row = 8 * (w * 4 + i) + (lane >> 3);
    int c = (lane & 7) ^ (((row >> 1) & 1) << 2);
    bsrc[i] = (const char*)wo + ((size_t)row * 1024 + n0) * 2 + c * 16;
  }
  auto issue_stage = [&](int t) {
    char* slot = smem + (t % K2_NS) * K2_SLOT;
#pragma unroll
    for (int i = 0; i < 2; ++i) dma16(asrc[i] + (size_t)t * 256, slot + (w * 2 + i) * 1024);
#pragma unroll
    for (int i = 0; i < 4; ++i) dma16(bsrc[i] + (size_t)t * 128 * 2048, slot + 8192 + (w * 4 + i) * 1024);
  };
  int aoff[2], boff[2][2][2];
#pragma unroll
  for (int kk = 0; kk < 2; ++kk) {
    int ks = 2 * w + kk;
    int row = lane & 31;
    int ch = 2 * ks + (lane >> 5);
    aoff[kk] = row * 256 + ((ch ^ (row & 15)) << 4);
#pragma unroll
    for (int nb = 0; nb < 2; ++nb)
#pragma unroll
      for (int t = 0; t < 2; ++t) {
        int r2 = 16 * ks + 8 * (lane >> 5) + 4 * t + ((lane & 15) >> 2);
        int ch2 = 4 * nb + 2 * ((lane >> 4) & 1) + ((lane & 3) >> 1);
        boff[nb][kk][t] = 8192 + r2 * 128 + ((ch2 ^ (((r2 >> 1) & 1) << 2)) << 4) + 8 * (lane & 1);
      }
  }
  f32x16 acc[2];
#pragma unroll
  for (int i = 0; i < 2; ++i)
#pragma unroll
    for (int r = 0; r < 16; ++r) acc[i][r] = 0.f;

#pragma unroll
  for (int t = 0; t < K2_NS - 1; ++t) issue_stage(t);

  static_for<K2_NT>([&](auto tc) {
    constexpr int t = decltype(tc)::value;
    constexpr int rem = K2_NT - 1 - t;
    constexpr int allowed = (rem >= K2_NS - 2) ? 6 * (K2_NS - 2) : 6 * rem;
    WAITV(allowed);
    BARRIER();
    if constexpr (t + K2_NS - 1 < K2_NT) issue_stage(t + K2_NS - 1);
    const char* slot = smem + (t % K2_NS) * K2_SLOT;
#pragma unroll
    for (int kk = 0; kk < 2; ++kk) {
      bf16x8 of = lds_rd128(slot + aoff[kk]);
#pragma unroll
      for (int nb = 0; nb < 2; ++nb) {
        bf16x8 wf = cat4(lds_tr(slot + boff[nb][kk][0]), lds_tr(slot + boff[nb][kk][1]));
        acc[nb] = __builtin_amdgcn_mfma_f32_32x32x16_bf16(wf, of, acc[nb], 0, 0, 0);
      }
    }
  });

  WAITL0();
  BARRIER();
  {
    int m = lane & 31;
#pragma unroll
    for (int nb = 0; nb < 2; ++nb)
#pragma unroll
      for (int g = 0; g < 4; ++g) {
        int s16 = 8 * nb + 2 * g + (lane >> 5);
        f32x4 v = {acc[nb][4 * g], acc[nb][4 * g + 1], acc[nb][4 * g + 2], acc[nb][4 * g + 3]};
        *(f32x4*)(smem + w * 8192 + m * 256 + ((s16 ^ (m & 15)) << 4)) = v;
      }
  }
  WAITL0();
  BARRIER();
  {
    int m = tid >> 3, c8 = tid & 7;
    f32x4 s0 = {0.f, 0.f, 0.f, 0.f}, s1 = {0.f, 0.f, 0.f, 0.f};
#pragma unroll
    for (int ww = 0; ww < 4; ++ww) {
      s0 += *(const f32x4*)(smem + ww * 8192 + m * 256 + (((2 * c8) ^ (m & 15)) << 4));
      s1 += *(const f32x4*)(smem + ww * 8192 + m * 256 + (((2 * c8 + 1) ^ (m & 15)) << 4));
    }
    u32x4 u;
    u[0] = pack_bf16(s0[0], s0[1]);
    u[1] = pack_bf16(s0[2], s0[3]);
    u[2] = pack_bf16(s1[0], s1[1]);
    u[3] = pack_bf16(s1[2], s1[3]);
    *(u32x4*)(out + (size_t)(m0 + m) * 1024 + n0 + 8 * c8) = u;
  }
}

extern "C" void kernel_launch(void* const* d_in, const int* in_sizes, int n_in, void* d_out, int out_size,
                              void* d_ws, size_t ws_size, hipStream_t stream) {
  const __hip_bfloat16* x = (const __hip_bfloat16*)d_in[0];
  const __hip_bfloat16* kc = (const __hip_bfloat16*)d_in[1];
  const __hip_bfloat16* vc = (const __hip_bfloat16*)d_in[2];
  const __hip_bfloat16* wq = (const __hip_bfloat16*)d_in[3];
  const __hip_bfloat16* wo = (const __hip_bfloat16*)d_in[4];
  __hip_bfloat16* o = (__hip_bfloat16*)d_ws;
  qattn_kernel<<<256, 256, 0, stream>>>(x, kc, vc, wq, o);
  oproj_kernel<<<256, 256, 0, stream>>>(o, wo, (__hip_bfloat16*)d_out);
}
```

```cpp
#include <hip/hip_runtime.h>
#include <hip/hip_bf16.h>
#include <utility>
#define ROT1 1
#define ROT2 1
#define PFWO 1

typedef __attribute__((ext_vector_type(8))) short bf16x8;
typedef __attribute__((ext_vector_type(4))) short bf16x4;
typedef __attribute__((ext_vector_type(4))) float f32x4;
typedef __attribute__((ext_vector_type(16))) float f32x16;
typedef __attribute__((ext_vector_type(4))) unsigned u32x4;
typedef __attribute__((ext_vector_type(2))) unsigned u32x2;
#define LDS_AS __attribute__((address_space(3)))

#define WAITV(n) asm volatile("s_waitcnt vmcnt(%0)" ::"n"(n) : "memory")
#define WAITL0() asm volatile("s_waitcnt lgkmcnt(0)" ::: "memory")
#define BARRIER() do { asm volatile("" ::: "memory"); __builtin_amdgcn_s_barrier(); asm volatile("" ::: "memory"); } while (0)

__device__ __forceinline__ unsigned pack_bf16(float lo, float hi) {
  typedef __attribute__((ext_vector_type(2))) float f32x2;
  typedef __attribute__((ext_vector_type(2))) __bf16 bf16x2;
  f32x2 v = {lo, hi};
  bf16x2 r = __builtin_convertvector(v, bf16x2);
  return __builtin_bit_cast(unsigned, r);
}
__device__ __forceinline__ unsigned lds_addr(const char* p) { return (unsigned)(size_t)(LDS_AS const char*)p; }
__device__ __forceinline__ void dma16(const char* gsrc, char* lds_base  ) {
  unsigned keep;
  unsigned dst = lds_addr(lds_base);
  asm volatile("s_mov_b32 %0, m0\n\ts_mov_b32 m0, %2\n\ts_nop 0\n\tglobal_load_lds_dwordx4 %1, off\n\ts_mov_b32 m0, %0"
               : "=&s"(keep) : "v"(gsrc), "s"(dst) : "memory");
}
__device__ __forceinline__ bf16x8 lds_rd128(const char* p) { return *(const bf16x8*)p; }
__device__ __forceinline__ bf16x4 lds_tr(const char* p) {
  return __builtin_amdgcn_ds_read_tr16_b64_v4i16((LDS_AS bf16x4*)p);
}
__device__ __forceinline__ bf16x8 cat4(bf16x4 a, bf16x4 b) {
  return __builtin_shufflevector(a, b, 0, 1, 2, 3, 4, 5, 6, 7);
}

template <int... I, class F>
__device__ __forceinline__ void static_for_impl(std::integer_sequence<int, I...>, F&& f) {
  (f(std::integral_constant<int, I>{}), ...);
}
template <int N, class F>
__device__ __forceinline__ void static_for(F&& f) { static_for_impl(std::make_integer_sequence<int, N>{}, f); }

constexpr int K1_NS = 6;
constexpr int K1_SLOT = 24576;
constexpr int K1_NT = 16;

__global__ __launch_bounds__(512) void qattn_kernel(const __hip_bfloat16* __restrict__ x,
                                                    const __hip_bfloat16* __restrict__ kc,
                                                    const __hip_bfloat16* __restrict__ vc,
                                                    const __hip_bfloat16* __restrict__ wq,
                                                    const __hip_bfloat16* __restrict__ wo,
                                                    __hip_bfloat16* __restrict__ o) {
  __shared__ __attribute__((aligned(1024))) char smem[K1_NS * K1_SLOT];
  const int tid = threadIdx.x, lane = tid & 63;
  const int w = __builtin_amdgcn_readfirstlane(tid >> 6);
  const int bid = blockIdx.x;
  const int h = (bid & 7) * 4 + ((bid >> 3) & 3);
  const int b = bid >> 5;

  const char* xsrc;
  const char* wsrc[2];
  {
    int row = 8 * w + (lane >> 3);
    int c = (lane & 7) ^ ((row >> 1) & 7);
    xsrc = (const char*)x + ((size_t)(b * 64 + row) * 1024) * 2 + c * 16;
  }
#pragma unroll
  for (int i = 0; i < 2; ++i) {
    int row = 4 * (w * 2 + i) + (lane >> 4);
    int c = (lane & 15) ^ ((row & 3) << 2);
    wsrc[i] = (const char*)wq + ((size_t)row * 4096 + h * 128) * 2 + c * 16;
  }
  const int rot1 = ROT1 ? ((bid >> 3) & 15) : 0;
  auto issue_stage = [&](int t) {
    char* slot = smem + (t % K1_NS) * K1_SLOT;
    const int tt = (t + rot1) & 15;
    dma16(xsrc + (size_t)tt * 128, slot + w * 1024);
#pragma unroll
    for (int i = 0; i < 2; ++i) dma16(wsrc[i] + (size_t)tt * 64 * 8192, slot + 8192 + (w * 2 + i) * 1024);
  };
  char* const vimg = smem + 4 * K1_SLOT;
  char* const kimg = smem + 5 * K1_SLOT;
  char* const qimg = smem + 0 * K1_SLOT;
  auto issue_v = [&]() {
    const char* vb = (const char*)vc + (size_t)(b * 32 + h) * 64 * 128 * 2;
#pragma unroll
    for (int i = 0; i < 2; ++i) {
      int p = w * 2 + i;
      int row = 4 * p + (lane >> 4);
      int c = (lane & 15) ^ ((row & 7) << 1);
      dma16(vb + row * 256 + c * 16, vimg + p * 1024);
    }
  };
  auto issue_k = [&]() {
    const char* kb = (const char*)kc + (size_t)(b * 32 + h) * 64 * 128 * 2;
#pragma unroll
    for (int i = 0; i < 2; ++i) {
      int p = w * 2 + i;
      int row = 4 * p + (lane >> 4);
      int c = (lane & 15) ^ (row & 15);
      dma16(kb + row * 256 + c * 16, kimg + p * 1024);
    }
  };

  const int wd = w >> 1, wqh = w & 1;
  int xoff[4], woff[4][2];
#pragma unroll
  for (int ks = 0; ks < 4; ++ks) {
    int xr = 32 * wqh + (lane & 31);
    int ch = 2 * ks + (lane >> 5);
    xoff[ks] = xr * 128 + ((ch ^ ((xr >> 1) & 7)) << 4);
#pragma unroll
    for (int t = 0; t < 2; ++t) {
      int row = 16 * ks + 8 * (lane >> 5) + 4 * t + ((lane & 15) >> 2);
      int ch2 = 4 * wd + 2 * ((lane >> 4) & 1) + ((lane & 3) >> 1);
      woff[ks][t] = 8192 + row * 256 + ((ch2 ^ ((row & 3) << 2)) << 4) + 8 * (lane & 1);
    }
  }

  f32x16 acc;
#pragma unroll
  for (int r = 0; r < 16; ++r) acc[r] = 0.f;

#pragma unroll
  for (int t = 0; t < K1_NS - 1; ++t) issue_stage(t);

  static_for<K1_NT>([&](auto tc) {
    constexpr int t = decltype(tc)::value;
    constexpr int allowed = (t <= 11) ? 12 : (t == 12) ? 11 : (t == 13) ? 10 : (t == 14) ? 7 : 4;
    WAITV(allowed);
    BARRIER();
    if constexpr (t + K1_NS - 1 < K1_NT) issue_stage(t + K1_NS - 1);
    else if constexpr (t == 11) issue_v();
    else if constexpr (t == 12) issue_k();
    const char* slot = smem + (t % K1_NS) * K1_SLOT;
#pragma unroll
    for (int ks = 0; ks < 4; ++ks) {
      bf16x8 xf = lds_rd128(slot + xoff[ks]);
      bf16x8 wf = cat4(lds_tr(slot + woff[ks][0]), lds_tr(slot + woff[ks][1]));
      acc = __builtin_amdgcn_mfma_f32_32x32x16_bf16(wf, xf, acc, 0, 0, 0);
    }
  });

  {
    int qrow = 32 * wqh + (lane & 31);
#pragma unroll
    for (int g = 0; g < 4; ++g) {
      int ch = 4 * wd + g;
      u32x2 v;
      v[0] = pack_bf16(acc[4 * g + 0], acc[4 * g + 1]);
      v[1] = pack_bf16(acc[4 * g + 2], acc[4 * g + 3]);
      *(u32x2*)(qimg + qrow * 256 + ((ch ^ (qrow & 15)) << 4) + 8 * (lane >> 5)) = v;
    }
  }
  asm volatile("s_waitcnt vmcnt(0) lgkmcnt(0)" ::: "memory");
  BARRIER();
  u32x4 pf0 = {0,0,0,0}, pf1 = {0,0,0,0}, pf2 = {0,0,0,0}, pf3 = {0,0,0,0};
  if (PFWO) {
    const char* pw = (const char*)wo + (size_t)bid * 32768 + w * 4096 + lane * 16;
    pf0 = *(const u32x4*)(pw); pf1 = *(const u32x4*)(pw + 1024);
    pf2 = *(const u32x4*)(pw + 2048); pf3 = *(const u32x4*)(pw + 3072);
  }

  const int l15 = lane & 15, g = lane >> 4;
  const int qw = w & 3, dh = w >> 2;
  bf16x8 qf[4];
  {
    int qr = 16 * qw + l15;
#pragma unroll
    for (int s = 0; s < 4; ++s) qf[s] = lds_rd128(qimg + qr * 256 + (((4 * s + g) ^ (qr & 15)) << 4));
  }
  f32x4 st[4];
#pragma unroll
  for (int kb = 0; kb < 4; ++kb) {
    st[kb] = (f32x4){0.f, 0.f, 0.f, 0.f};
    int kr = 16 * kb + l15;
#pragma unroll
    for (int s = 0; s < 4; ++s) {
      bf16x8 kf = lds_rd128(kimg + kr * 256 + (((4 * s + g) ^ (kr & 15)) << 4));
      st[kb] = __builtin_amdgcn_mfma_f32_16x16x32_bf16(kf, qf[s], st[kb], 0, 0, 0);
    }
  }
  const float scale = 0.08838834764831845f;
  float mx = -3.0e38f;
#pragma unroll
  for (int kb = 0; kb < 4; ++kb)
#pragma unroll
    for (int r = 0; r < 4; ++r) { st[kb][r] *= scale; mx = fmaxf(mx, st[kb][r]); }
  mx = fmaxf(mx, __shfl_xor(mx, 16));
  mx = fmaxf(mx, __shfl_xor(mx, 32));
  float sum = 0.f;
#pragma unroll
  for (int kb = 0; kb < 4; ++kb)
#pragma unroll
    for (int r = 0; r < 4; ++r) { float e = __expf(st[kb][r] - mx); st[kb][r] = e; sum += e; }
  sum += __shfl_xor(sum, 16);
  sum += __shfl_xor(sum, 32);
  const float rinv = 1.0f / sum;
  bf16x8 pf[2];
#pragma unroll
  for (int s2 = 0; s2 < 2; ++s2) {
    u32x4 u;
    u[0] = pack_bf16(st[2 * s2][0] * rinv, st[2 * s2][1] * rinv);
    u[1] = pack_bf16(st[2 * s2][2] * rinv, st[2 * s2][3] * rinv);
    u[2] = pack_bf16(st[2 * s2 + 1][0] * rinv, st[2 * s2 + 1][1] * rinv);
    u[3] = pack_bf16(st[2 * s2 + 1][2] * rinv, st[2 * s2 + 1][3] * rinv);
    pf[s2] = __builtin_bit_cast(bf16x8, u);
  }
  __hip_bfloat16* orow = o + (size_t)(b * 64 + 16 * qw + l15) * 4096 + h * 128;
#pragma unroll
  for (int dd = 0; dd < 4; ++dd) {
    int db = 4 * dh + dd;
    f32x4 oa = {0.f, 0.f, 0.f, 0.f};
#pragma unroll
    for (int s2 = 0; s2 < 2; ++s2) {
      bf16x4 vv[2];
#pragma unroll
      for (int t = 0; t < 2; ++t) {
        int row = 32 * s2 + 16 * t + 4 * g + (l15 >> 2);
        int ch = 2 * db + ((lane & 3) >> 1);
        vv[t] = lds_tr(vimg + row * 256 + ((ch ^ ((row & 7) << 1)) << 4) + 8 * (lane & 1));
      }
      oa = __builtin_amdgcn_mfma_f32_16x16x32_bf16(cat4(vv[0], vv[1]), pf[s2], oa, 0, 0, 0);
    }
    u32x2 v;
    v[0] = pack_bf16(oa[0], oa[1]);
    v[1] = pack_bf16(oa[2], oa[3]);
    *(u32x2*)(orow + 16 * db + 4 * g) = v;
  }
  asm volatile("" :: "v"(pf0), "v"(pf1), "v"(pf2), "v"(pf3));
}

constexpr int K2_NS = 6;
constexpr int K2_SLOT = 24576;
constexpr int K2_NT = 32;

__global__ __launch_bounds__(512) void oproj_kernel(const __hip_bfloat16* __restrict__ o,
                                                    const __hip_bfloat16* __restrict__ wo,
                                                    __hip_bfloat16* __restrict__ out) {
  __shared__ __attribute__((aligned(1024))) char smem[K2_NS * K2_SLOT];
  const int tid = threadIdx.x, lane = tid & 63;
  const int w = __builtin_amdgcn_readfirstlane(tid >> 6);
  const int bid = blockIdx.x;
  const int xj = bid & 7, idx = bid >> 3;
  const int mt = 8 * (xj >> 2) + (idx >> 2);
  const int nt = 4 * (xj & 3) + (idx & 3);
  const int m0 = 32 * mt, n0 = 64 * nt;

  const char* asrc;
  const char* bsrc[2];
  {
    int row = 4 * w + (lane >> 4);
    int c = (lane & 15) ^ (row & 15);
    asrc = (const char*)o + ((size_t)(m0 + row) * 4096) * 2 + c * 16;
  }
#pragma unroll
  for (int i = 0; i < 2; ++i) {
    int row = 8 * (w * 2 + i) + (lane >> 3);
    int c = (lane & 7) ^ (((row >> 1) & 1) << 2);
    bsrc[i] = (const char*)wo + ((size_t)row * 1024 + n0) * 2 + c * 16;
  }
  const int rot2 = ROT2 ? ((idx + 5 * xj) & 31) : 0;
  auto issue_stage = [&](int t) {
    char* slot = smem + (t % K2_NS) * K2_SLOT;
    const int tt = (t + rot2) & 31;
    dma16(asrc + (size_t)tt * 256, slot + w * 1024);
#pragma unroll
    for (int i = 0; i < 2; ++i) dma16(bsrc[i] + (size_t)tt * 128 * 2048, slot + 8192 + (w * 2 + i) * 1024);
  };
  int aoff, boff[2][2];
  {
    int ks = w;
    int row = lane & 31;
    int ch = 2 * ks + (lane >> 5);
    aoff = row * 256 + ((ch ^ (row & 15)) << 4);
#pragma unroll
    for (int nb = 0; nb < 2; ++nb)
#pragma unroll
      for (int t = 0; t < 2; ++t) {
        int r2 = 16 * ks + 8 * (lane >> 5) + 4 * t + ((lane & 15) >> 2);
        int ch2 = 4 * nb + 2 * ((lane >> 4) & 1) + ((lane & 3) >> 1);
        boff[nb][t] = 8192 + r2 * 128 + ((ch2 ^ (((r2 >> 1) & 1) << 2)) << 4) + 8 * (lane & 1);
      }
  }
  f32x16 acc[2];
#pragma unroll
  for (int i = 0; i < 2; ++i)
#pragma unroll
    for (int r = 0; r < 16; ++r) acc[i][r] = 0.f;

#pragma unroll
  for (int t = 0; t < K2_NS - 1; ++t) issue_stage(t);

  static_for<K2_NT>([&](auto tc) {
    constexpr int t = decltype(tc)::value;
    constexpr int rem = K2_NT - 1 - t;
    constexpr int allowed = (rem >= K2_NS - 2) ? 3 * (K2_NS - 2) : 3 * rem;
    WAITV(allowed);
    BARRIER();
    if constexpr (t + K2_NS - 1 < K2_NT) issue_stage(t + K2_NS - 1);
    const char* slot = smem + (t % K2_NS) * K2_SLOT;
    bf16x8 of = lds_rd128(slot + aoff);
#pragma unroll
    for (int nb = 0; nb < 2; ++nb) {
      bf16x8 wf = cat4(lds_tr(slot + boff[nb][0]), lds_tr(slot + boff[nb][1]));
      acc[nb] = __builtin_amdgcn_mfma_f32_32x32x16_bf16(wf, of, acc[nb], 0, 0, 0);
    }
  });

  WAITL0();
  BARRIER();
  {
    int m = lane & 31;
#pragma unroll
    for (int nb = 0; nb < 2; ++nb)
#pragma unroll
      for (int g = 0; g < 4; ++g) {
        int s16 = 8 * nb + 2 * g + (lane >> 5);
        f32x4 v = {acc[nb][4 * g], acc[nb][4 * g + 1], acc[nb][4 * g + 2], acc[nb][4 * g + 3]};
        *(f32x4*)(smem + w * 8192 + m * 256 + ((s16 ^ (m & 15)) << 4)) = v;
      }
  }
  WAITL0();
  BARRIER();
  if (tid < 256) {
    int m = tid >> 3, c8 = tid & 7;
    f32x4 s0 = {0.f, 0.f, 0.f, 0.f}, s1 = {0.f, 0.f, 0.f, 0.f};
#pragma unroll
    for (int ww = 0; ww < 8; ++ww) {
      s0 += *(const f32x4*)(smem + ww * 8192 + m * 256 + (((2 * c8) ^ (m & 15)) << 4));
      s1 += *(const f32x4*)(smem + ww * 8192 + m * 256 + (((2 * c8 + 1) ^ (m & 15)) << 4));
    }
    u32x4 u;
    u[0] = pack_bf16(s0[0], s0[1]);
    u[1] = pack_bf16(s0[2], s0[3]);
    u[2] = pack_bf16(s1[0], s1[1]);
    u[3] = pack_bf16(s1[2], s1[3]);
    *(u32x4*)(out + (size_t)(m0 + m) * 1024 + n0 + 8 * c8) = u;
  }
}

extern "C" void kernel_launch(void* const* d_in, const int* in_sizes, int n_in, void* d_out, int out_size,
                              void* d_ws, size_t ws_size, hipStream_t stream) {
  const __hip_bfloat16* x = (const __hip_bfloat16*)d_in[0];
  const __hip_bfloat16* kc = (const __hip_bfloat16*)d_in[1];
  const __hip_bfloat16* vc = (const __hip_bfloat16*)d_in[2];
  const __hip_bfloat16* wq = (const __hip_bfloat16*)d_in[3];
  const __hip_bfloat16* wo = (const __hip_bfloat16*)d_in[4];
  __hip_bfloat16* o = (__hip_bfloat16*)d_ws;
  qattn_kernel<<<256, 512, 0, stream>>>(x, kc, vc, wq, wo, o);
  oproj_kernel<<<256, 512, 0, stream>>>(o, wo, (__hip_bfloat16*)d_out);
}
```

```cpp
#include <hip/hip_runtime.h>
#include <hip/hip_bf16.h>
#include <utility>

typedef __attribute__((ext_vector_type(8))) short bf16x8;
typedef __attribute__((ext_vector_type(4))) short bf16x4;
typedef __attribute__((ext_vector_type(4))) float f32x4;
typedef __attribute__((ext_vector_type(16))) float f32x16;
typedef __attribute__((ext_vector_type(4))) unsigned u32x4;
typedef __attribute__((ext_vector_type(2))) unsigned u32x2;
#define LDS_AS __attribute__((address_space(3)))

#define WAITV(n) asm volatile("s_waitcnt vmcnt(%0)" ::"n"(n) : "memory")
#define WAITL0() asm volatile("s_waitcnt lgkmcnt(0)" ::: "memory")
#define BARRIER() do { asm volatile("" ::: "memory"); __builtin_amdgcn_s_barrier(); asm volatile("" ::: "memory"); } while (0)

__device__ __forceinline__ unsigned pack_bf16(float lo, float hi) {
  typedef __attribute__((ext_vector_type(2))) float f32x2;
  typedef __attribute__((ext_vector_type(2))) __bf16 bf16x2;
  f32x2 v = {lo, hi};
  bf16x2 r = __builtin_convertvector(v, bf16x2);
  return __builtin_bit_cast(unsigned, r);
}
__device__ __forceinline__ unsigned lds_addr(const char* p) { return (unsigned)(size_t)(LDS_AS const char*)p; }
__device__ __forceinline__ void dma16(const char* gsrc, char* lds_base  ) {
  unsigned keep;
  unsigned dst = lds_addr(lds_base);
  asm volatile("s_mov_b32 %0, m0\n\ts_mov_b32 m0, %2\n\ts_nop 0\n\tglobal_load_lds_dwordx4 %1, off\n\ts_mov_b32 m0, %0"
               : "=&s"(keep) : "v"(gsrc), "s"(dst) : "memory");
}
__device__ __forceinline__ bf16x8 lds_rd128(const char* p) { return *(const bf16x8*)p; }
__device__ __forceinline__ bf16x4 lds_tr(const char* p) {
  return __builtin_amdgcn_ds_read_tr16_b64_v4i16((LDS_AS bf16x4*)p);
}
__device__ __forceinline__ bf16x8 cat4(bf16x4 a, bf16x4 b) {
  return __builtin_shufflevector(a, b, 0, 1, 2, 3, 4, 5, 6, 7);
}

template <int... I, class F>
__device__ __forceinline__ void static_for_impl(std::integer_sequence<int, I...>, F&& f) {
  (f(std::integral_constant<int, I>{}), ...);
}
template <int N, class F>
__device__ __forceinline__ void static_for(F&& f) { static_for_impl(std::make_integer_sequence<int, N>{}, f); }

constexpr int K1_NS = 6;
constexpr int K1_SLOT = 24576;
constexpr int K1_NT = 16;

__global__ __launch_bounds__(512) void qattn_kernel(const __hip_bfloat16* __restrict__ x,
                                                    const __hip_bfloat16* __restrict__ kc,
                                                    const __hip_bfloat16* __restrict__ vc,
                                                    const __hip_bfloat16* __restrict__ wq,
                                                    __hip_bfloat16* __restrict__ o) {
  __shared__ __attribute__((aligned(1024))) char smem[K1_NS * K1_SLOT];
  const int tid = threadIdx.x, lane = tid & 63;
  const int w = __builtin_amdgcn_readfirstlane(tid >> 6);
  const int bid = blockIdx.x;
  const int h = (bid & 7) * 4 + ((bid >> 3) & 3);
  const int b = bid >> 5;

  const char* xsrc;
  const char* wsrc[2];
  {
    int row = 8 * w + (lane >> 3);
    int c = (lane & 7) ^ ((row >> 1) & 7);
    xsrc = (const char*)x + ((size_t)(b * 64 + row) * 1024) * 2 + c * 16;
  }
#pragma unroll
  for (int i = 0; i < 2; ++i) {
    int row = 4 * (w * 2 + i) + (lane >> 4);
    int c = (lane & 15) ^ ((row & 3) << 2);
    wsrc[i] = (const char*)wq + ((size_t)row * 4096 + h * 128) * 2 + c * 16;
  }
  const int rot1 = (bid >> 3) & 15;
  auto issue_stage = [&](int t) {
    char* slot = smem + (t % K1_NS) * K1_SLOT;
    const int tt = (t + rot1) & 15;
    dma16(xsrc + (size_t)tt * 128, slot + w * 1024);
#pragma unroll
    for (int i = 0; i < 2; ++i) dma16(wsrc[i] + (size_t)tt * 64 * 8192, slot + 8192 + (w * 2 + i) * 1024);
  };
  char* const vimg = smem + 4 * K1_SLOT;
  char* const kimg = smem + 5 * K1_SLOT;
  char* const qimg = smem + 0 * K1_SLOT;
  auto issue_v = [&]() {
    const char* vb = (const char*)vc + (size_t)(b * 32 + h) * 64 * 128 * 2;
#pragma unroll
    for (int i = 0; i < 2; ++i) {
      int p = w * 2 + i;
      int row = 4 * p + (lane >> 4);
      int c = (lane & 15) ^ ((row & 7) << 1);
      dma16(vb + row * 256 + c * 16, vimg + p * 1024);
    }
  };
  auto issue_k = [&]() {
    const char* kb = (const char*)kc + (size_t)(b * 32 + h) * 64 * 128 * 2;
#pragma unroll
    for (int i = 0; i < 2; ++i) {
      int p = w * 2 + i;
      int row = 4 * p + (lane >> 4);
      int c = (lane & 15) ^ (row & 15);
      dma16(kb + row * 256 + c * 16, kimg + p * 1024);
    }
  };

  const int wd = w >> 1, wqh = w & 1;
  int xoff[4], woff[4][2];
#pragma unroll
  for (int ks = 0; ks < 4; ++ks) {
    int xr = 32 * wqh + (lane & 31);
    int ch = 2 * ks + (lane >> 5);
    xoff[ks] = xr * 128 + ((ch ^ ((xr >> 1) & 7)) << 4);
#pragma unroll
    for (int t = 0; t < 2; ++t) {
      int row = 16 * ks + 8 * (lane >> 5) + 4 * t + ((lane & 15) >> 2);
      int ch2 = 4 * wd + 2 * ((lane >> 4) & 1) + ((lane & 3) >> 1);
      woff[ks][t] = 8192 + row * 256 + ((ch2 ^ ((row & 3) << 2)) << 4) + 8 * (lane & 1);
    }
  }

  f32x16 acc;
#pragma unroll
  for (int r = 0; r < 16; ++r) acc[r] = 0.f;

#pragma unroll
  for (int t = 0; t < K1_NS - 1; ++t) issue_stage(t);

  static_for<K1_NT>([&](auto tc) {
    constexpr int t = decltype(tc)::value;
    constexpr int allowed = (t <= 11) ? 12 : (t == 12) ? 11 : (t == 13) ? 10 : (t == 14) ? 7 : 4;
    WAITV(allowed);
    BARRIER();
    if constexpr (t + K1_NS - 1 < K1_NT) issue_stage(t + K1_NS - 1);
    else if constexpr (t == 11) issue_v();
    else if constexpr (t == 12) issue_k();
    const char* slot = smem + (t % K1_NS) * K1_SLOT;
#pragma unroll
    for (int ks = 0; ks < 4; ++ks) {
      bf16x8 xf = lds_rd128(slot + xoff[ks]);
      bf16x8 wf = cat4(lds_tr(slot + woff[ks][0]), lds_tr(slot + woff[ks][1]));
      acc = __builtin_amdgcn_mfma_f32_32x32x16_bf16(wf, xf, acc, 0, 0, 0);
    }
  });

  {
    int qrow = 32 * wqh + (lane & 31);
#pragma unroll
    for (int g = 0; g < 4; ++g) {
      int ch = 4 * wd + g;
      u32x2 v;
      v[0] = pack_bf16(acc[4 * g + 0], acc[4 * g + 1]);
      v[1] = pack_bf16(acc[4 * g + 2], acc[4 * g + 3]);
      *(u32x2*)(qimg + qrow * 256 + ((ch ^ (qrow & 15)) << 4) + 8 * (lane >> 5)) = v;
    }
  }
  asm volatile("s_waitcnt vmcnt(0) lgkmcnt(0)" ::: "memory");
  BARRIER();
  const int l15 = lane & 15, g = lane >> 4;
  const int qw = w & 3, dh = w >> 2;
  bf16x8 qf[4];
  {
    int qr = 16 * qw + l15;
#pragma unroll
    for (int s = 0; s < 4; ++s) qf[s] = lds_rd128(qimg + qr * 256 + (((4 * s + g) ^ (qr & 15)) << 4));
  }
  f32x4 st[4];
#pragma unroll
  for (int kb = 0; kb < 4; ++kb) {
    st[kb] = (f32x4){0.f, 0.f, 0.f, 0.f};
    int kr = 16 * kb + l15;
#pragma unroll
    for (int s = 0; s < 4; ++s) {
      bf16x8 kf = lds_rd128(kimg + kr * 256 + (((4 * s + g) ^ (kr & 15)) << 4));
      st[kb] = __builtin_amdgcn_mfma_f32_16x16x32_bf16(kf, qf[s], st[kb], 0, 0, 0);
    }
  }
  const float scale = 0.08838834764831845f;
  float mx = -3.0e38f;
#pragma unroll
  for (int kb = 0; kb < 4; ++kb)
#pragma unroll
    for (int r = 0; r < 4; ++r) { st[kb][r] *= scale; mx = fmaxf(mx, st[kb][r]); }
  mx = fmaxf(mx, __shfl_xor(mx, 16));
  mx = fmaxf(mx, __shfl_xor(mx, 32));
  float sum = 0.f;
#pragma unroll
  for (int kb = 0; kb < 4; ++kb)
#pragma unroll
    for (int r = 0; r < 4; ++r) { float e = __expf(st[kb][r] - mx); st[kb][r] = e; sum += e; }
  sum += __shfl_xor(sum, 16);
  sum += __shfl_xor(sum, 32);
  const float rinv = 1.0f / sum;
  bf16x8 pf[2];
#pragma unroll
  for (int s2 = 0; s2 < 2; ++s2) {
    u32x4 u;
    u[0] = pack_bf16(st[2 * s2][0] * rinv, st[2 * s2][1] * rinv);
    u[1] = pack_bf16(st[2 * s2][2] * rinv, st[2 * s2][3] * rinv);
    u[2] = pack_bf16(st[2 * s2 + 1][0] * rinv, st[2 * s2 + 1][1] * rinv);
    u[3] = pack_bf16(st[2 * s2 + 1][2] * rinv, st[2 * s2 + 1][3] * rinv);
    pf[s2] = __builtin_bit_cast(bf16x8, u);
  }
  __hip_bfloat16* orow = o + (size_t)(b * 64 + 16 * qw + l15) * 4096 + h * 128;
#pragma unroll
  for (int dd = 0; dd < 4; ++dd) {
    int db = 4 * dh + dd;
    f32x4 oa = {0.f, 0.f, 0.f, 0.f};
#pragma unroll
    for (int s2 = 0; s2 < 2; ++s2) {
      bf16x4 vv[2];
#pragma unroll
      for (int t = 0; t < 2; ++t) {
        int row = 32 * s2 + 16 * t + 4 * g + (l15 >> 2);
        int ch = 2 * db + ((lane & 3) >> 1);
        vv[t] = lds_tr(vimg + row * 256 + ((ch ^ ((row & 7) << 1)) << 4) + 8 * (lane & 1));
      }
      oa = __builtin_amdgcn_mfma_f32_16x16x32_bf16(cat4(vv[0], vv[1]), pf[s2], oa, 0, 0, 0);
    }
    u32x2 v;
    v[0] = pack_bf16(oa[0], oa[1]);
    v[1] = pack_bf16(oa[2], oa[3]);
    *(u32x2*)(orow + 16 * db + 4 * g) = v;
  }
}

constexpr int K2_NS = 6;
constexpr int K2_SLOT = 24576;
constexpr int K2_NT = 32;

__global__ __launch_bounds__(512) void oproj_kernel(const __hip_bfloat16* __restrict__ o,
                                                    const __hip_bfloat16* __restrict__ wo,
                                                    __hip_bfloat16* __restrict__ out) {
  __shared__ __attribute__((aligned(1024))) char smem[K2_NS * K2_SLOT];
  const int tid = threadIdx.x, lane = tid & 63;
  const int w = __builtin_amdgcn_readfirstlane(tid >> 6);
  const int bid = blockIdx.x;
  const int xj = bid & 7, idx = bid >> 3;
  const int mt = 8 * (xj >> 2) + (idx >> 2);
  const int nt = 4 * (xj & 3) + (idx & 3);
  const int m0 = 32 * mt, n0 = 64 * nt;

  const char* asrc;
  const char* bsrc[2];
  {
    int row = 4 * w + (lane >> 4);
    int c = (lane & 15) ^ (row & 15);
    asrc = (const char*)o + ((size_t)(m0 + row) * 4096) * 2 + c * 16;
  }
#pragma unroll
  for (int i = 0; i < 2; ++i) {
    int row = 8 * (w * 2 + i) + (lane >> 3);
    int c = (lane & 7) ^ (((row >> 1) & 1) << 2);
    bsrc[i] = (const char*)wo + ((size_t)row * 1024 + n0) * 2 + c * 16;
  }
  const int rot2 = (idx + 5 * xj) & 31;
  auto issue_stage = [&](int t) {
    char* slot = smem + (t % K2_NS) * K2_SLOT;
    const int tt = (t + rot2) & 31;
    dma16(asrc + (size_t)tt * 256, slot + w * 1024);
#pragma unroll
    for (int i = 0; i < 2; ++i) dma16(bsrc[i] + (size_t)tt * 128 * 2048, slot + 8192 + (w * 2 + i) * 1024);
  };
  int aoff, boff[2][2];
  {
    int ks = w;
    int row = lane & 31;
    int ch = 2 * ks + (lane >> 5);
    aoff = row * 256 + ((ch ^ (row & 15)) << 4);
#pragma unroll
    for (int nb = 0; nb < 2; ++nb)
#pragma unroll
      for (int t = 0; t < 2; ++t) {
        int r2 = 16 * ks + 8 * (lane >> 5) + 4 * t + ((lane & 15) >> 2);
        int ch2 = 4 * nb + 2 * ((lane >> 4) & 1) + ((lane & 3) >> 1);
        boff[nb][t] = 8192 + r2 * 128 + ((ch2 ^ (((r2 >> 1) & 1) << 2)) << 4) + 8 * (lane & 1);
      }
  }
  f32x16 acc[2];
#pragma unroll
  for (int i = 0; i < 2; ++i)
#pragma unroll
    for (int r = 0; r < 16; ++r) acc[i][r] = 0.f;

#pragma unroll
  for (int t = 0; t < K2_NS - 1; ++t) issue_stage(t);

  static_for<K2_NT>([&](auto tc) {
    constexpr int t = decltype(tc)::value;
    constexpr int rem = K2_NT - 1 - t;
    constexpr int allowed = (rem >= K2_NS - 2) ? 3 * (K2_NS - 2) : 3 * rem;
    WAITV(allowed);
    BARRIER();
    if constexpr (t + K2_NS - 1 < K2_NT) issue_stage(t + K2_NS - 1);
    const char* slot = smem + (t % K2_NS) * K2_SLOT;
    bf16x8 of = lds_rd128(slot + aoff);
#pragma unroll
    for (int nb = 0; nb < 2; ++nb) {
      bf16x8 wf = cat4(lds_tr(slot + boff[nb][0]), lds_tr(slot + boff[nb][1]));
      acc[nb] = __builtin_amdgcn_mfma_f32_32x32x16_bf16(wf, of, acc[nb], 0, 0, 0);
    }
  });

  WAITL0();
  BARRIER();
  {
    int m = lane & 31;
#pragma unroll
    for (int nb = 0; nb < 2; ++nb)
#pragma unroll
      for (int g = 0; g < 4; ++g) {
        int s16 = 8 * nb + 2 * g + (lane >> 5);
        f32x4 v = {acc[nb][4 * g], acc[nb][4 * g + 1], acc[nb][4 * g + 2], acc[nb][4 * g + 3]};
        *(f32x4*)(smem + w * 8192 + m * 256 + ((s16 ^ (m & 15)) << 4)) = v;
      }
  }
  WAITL0();
  BARRIER();
  if (tid < 256) {
    int m = tid >> 3, c8 = tid & 7;
    f32x4 s0 = {0.f, 0.f, 0.f, 0.f}, s1 = {0.f, 0.f, 0.f, 0.f};
#pragma unroll
    for (int ww = 0; ww < 8; ++ww) {
      s0 += *(const f32x4*)(smem + ww * 8192 + m * 256 + (((2 * c8) ^ (m & 15)) << 4));
      s1 += *(const f32x4*)(smem + ww * 8192 + m * 256 + (((2 * c8 + 1) ^ (m & 15)) << 4));
    }
    u32x4 u;
    u[0] = pack_bf16(s0[0], s0[1]);
    u[1] = pack_bf16(s0[2], s0[3]);
    u[2] = pack_bf16(s1[0], s1[1]);
    u[3] = pack_bf16(s1[2], s1[3]);
    *(u32x4*)(out + (size_t)(m0 + m) * 1024 + n0 + 8 * c8) = u;
  }
}

extern "C" void kernel_launch(void* const* d_in, const int* in_sizes, int n_in, void* d_out, int out_size,
                              void* d_ws, size_t ws_size, hipStream_t stream) {
  const __hip_bfloat16* x = (const __hip_bfloat16*)d_in[0];
  const __hip_bfloat16* kc = (const __hip_bfloat16*)d_in[1];
  const __hip_bfloat16* vc = (const __hip_bfloat16*)d_in[2];
  const __hip_bfloat16* wq = (const __hip_bfloat16*)d_in[3];
  const __hip_bfloat16* wo = (const __hip_bfloat16*)d_in[4];
  __hip_bfloat16* o = (__hip_bfloat16*)d_ws;
  qattn_kernel<<<256, 512, 0, stream>>>(x, kc, vc, wq, o);
  oproj_kernel<<<256, 512, 0, stream>>>(o, wo, (__hip_bfloat16*)d_out);
}
```
